```python
import torch

def _softmax(x, axis=-1):
    return torch.softmax(x, dim=axis)
def _gelu(x):
    return torch.nn.functional.gelu(x, approximate="tanh")
def _silu(x):
    return torch.nn.functional.silu(x)
def _sigmoid(x):
    return torch.sigmoid(x)
def _layernorm(x, axis=-1, eps=1e-5):
    m = x.mean(dim=axis, keepdim=True)
    v = x.var(dim=axis, keepdim=True, unbiased=False)
    return (x - m) / torch.sqrt(v + eps)
def _scatter_add(tgt, idx, src, axis=0):
    return tgt.clone().index_add_(axis, idx, src)
def _segment_sum(src, seg, n):
    out = torch.zeros((n,) + src.shape[1:], dtype=src.dtype)
    return out.index_add_(0, seg, src)
def _pad(x, pw):
    flat = tuple(v for pair in reversed(pw) for v in pair)
    return torch.nn.functional.pad(x, flat)

kernel_name = 'standalone__paged_block_table_kv_pagedattention__sampling'

def setup_inputs():

    _w = (lambda a: (lambda t: t.to(torch.bfloat16) if t.is_floating_point() else t)(torch.as_tensor(a)))
    B, Hq, Hkv, Sq, Skv, D = 8, 32, 8, 8, 8192, 128
    bs = 16
    nbps = max(1, Skv // bs)
    num_phys = B * nbps + 4
    bt = torch.randperm(num_phys)[: B * nbps].reshape(B, nbps).to(torch.int64)
    cl = (nbps * bs - (torch.arange(1, B + 1, dtype=torch.int64) * 2654435761) % max(1, nbps * bs // 2)).to(torch.int64)
    return {
        "q": _w((lambda *s: torch.randn(*s, dtype=torch.bfloat16) * 0.8)(B, Hq, Sq, D)),
        "k_cache": _w((lambda *s: torch.randn(*s, dtype=torch.bfloat16) * 0.8)(num_phys, bs, Hkv, D)),
        "v_cache": _w((lambda *s: torch.randn(*s, dtype=torch.bfloat16) * 0.05)(num_phys, bs, Hkv, D)),
        "block_table": _w(bt),
        "context_lens": _w(cl),
    }

def reference(q, k_cache, v_cache, block_table, context_lens):
    B, Hq, Sq, D = q.shape
    bs = k_cache.shape[1]
    Hkv = k_cache.shape[2]
    nbps = block_table.shape[1]
    Skv = nbps * bs
    bt = block_table.reshape(-1)
    k = (lambda x, *d: x.permute(*d))(k_cache[bt].reshape(B, Skv, Hkv, D), 0, 2, 1, 3)
    v = (lambda x, *d: x.permute(*d))(v_cache[bt].reshape(B, Skv, Hkv, D), 0, 2, 1, 3)
    g = Hq // Hkv
    qg = q.reshape(B, Hkv, g, Sq, D)
    scores = torch.einsum("bhgqd,bhkd->bhgqk", qg, k) * (D ** -0.5)
    pos = torch.arange(Skv)
    msk = pos[None, :] < context_lens[:, None]
    scores = torch.where(msk[:, None, None, None, :], scores, -1e9)
    p = _softmax(scores, axis=-1)
    out = torch.einsum("bhgqk,bhkd->bhgqd", p, v)
    return out.reshape(B, Hq, Sq, D)
```

```cpp
#include <hip/hip_runtime.h>
#include <hip/hip_bf16.h>
#include <stdint.h>

namespace {

constexpr int B = 8, HQ = 32, HKV = 8, SQ = 8, D = 128, PAGE = 16, NBPS = 512, NPAGES = 4100;
constexpr int SKV = NBPS * PAGE;
constexpr int M = (HQ / HKV) * SQ;
constexpr int NWG = 256, WAVES = 4, NT = (WAVES + 1) * 64;
constexpr int WGPH = NWG / (HKV / 2);
constexpr int WPH = WGPH * 2;
constexpr int SLOTS_PER_HEAD = WGPH + B;
constexpr int PAGE_BYTES = PAGE * HKV * D * 2;
constexpr float C2 = 0.088388347648318440f * 1.4426950408889634f;

constexpr int FLAG_STRIDE = 256;
constexpr int SLOT_BYTES = M * D * 2, UNIT_BYTES = 64 * 8;
constexpr int HEAD_STRIDE = (SLOTS_PER_HEAD + 1) * SLOT_BYTES;
constexpr size_t WS_O = 0;
constexpr size_t WS_ML = (size_t)HKV * HEAD_STRIDE;
constexpr size_t WS_FLAG = WS_ML + (size_t)HKV * SLOTS_PER_HEAD * M * 8;
constexpr size_t WS_EPOCH = WS_FLAG + (HKV / 2) * WGPH * FLAG_STRIDE;
constexpr size_t WS_BYTES = WS_EPOCH + (HKV / 2) * 256;
static_assert(WS_BYTES <= ((size_t)256 << 20), "workspace layout");
constexpr int AUX_SC1 = 16;
constexpr int CMB = 12;
constexpr unsigned SPIN_CAP = 1u << 18;
constexpr float PACE_TILE = 1.75f, PACE_LEAD = 262.f, PACE_SPREAD = 200.f;
constexpr unsigned PACE_FIRST = 200;

constexpr int WAVE_LDS = 16384;
constexpr int LDS_ML = WAVES * WAVE_LDS;
constexpr int LDS_BYTES = LDS_ML + WAVES * 256;

using bf16x8 = __attribute__((ext_vector_type(8))) __bf16;
using bf16x4 = __attribute__((ext_vector_type(4))) __bf16;
using f32x16 = __attribute__((ext_vector_type(16))) float;
using f32x4 = __attribute__((ext_vector_type(4))) float;
using f32x2 = __attribute__((ext_vector_type(2))) float;
using u32x4 = __attribute__((ext_vector_type(4))) unsigned;
using u32x2 = __attribute__((ext_vector_type(2))) unsigned;
using gu64 = __attribute__((address_space(1))) unsigned long long;
#define LDS_AS __attribute__((address_space(3)))

__device__ __forceinline__ int rfl(int x) { return __builtin_amdgcn_readfirstlane(x); }
__device__ __forceinline__ float ex2(float x) { return __builtin_amdgcn_exp2f(x); }

__device__ __forceinline__ float swap32_max(float x) {
  auto t = __builtin_amdgcn_permlane32_swap(__float_as_uint(x), __float_as_uint(x), false, false);
  return fmaxf(__uint_as_float(t[0]), __uint_as_float(t[1]));
}
__device__ __forceinline__ float swap32_sum(float x) {
  auto t = __builtin_amdgcn_permlane32_swap(__float_as_uint(x), __float_as_uint(x), false, false);
  return __uint_as_float(t[0]) + __uint_as_float(t[1]);
}

struct Part { int len, nt, start, zeroq, total; unsigned tstart; };
__device__ __forceinline__ Part partition(const int* __restrict__ lens, int lane) {
  Part p;
  const int cl = lens[lane & 7];
  __builtin_amdgcn_sched_barrier(0);
  p.tstart = (unsigned)__builtin_amdgcn_s_memrealtime();
  __builtin_amdgcn_sched_barrier(0);
  p.zeroq = cl <= 0;
  p.len = cl <= 0 ? SKV : min(cl, SKV);
  p.nt = (p.len + 31) >> 5;
  int x = p.nt;
  x += __builtin_amdgcn_update_dpp(0, x, 0x111, 0xf, 0xf, true);
  x += __builtin_amdgcn_update_dpp(0, x, 0x112, 0xf, 0xf, true);
  x += __builtin_amdgcn_update_dpp(0, x, 0x114, 0xf, 0xf, true);
  const int T = __builtin_amdgcn_readlane(x, 7);
  const float r = (float)(WPH - B) * __builtin_amdgcn_rcpf((float)T);
  p.start = (lane & 7) + min(WPH - B, (int)((float)(x - p.nt) * r + 0.5f));
  p.total = T;
  return p;
}
__device__ __forceinline__ int batch_of_wave(const Part& p, int lane, int u) {
  return __popcll(__ballot(lane < B && p.start <= u)) - 1;
}

__device__ __forceinline__ void store_partial(char* p, f32x4 v) {
  const bf16x4 r = {(__bf16)v[0], (__bf16)v[1], (__bf16)v[2], (__bf16)v[3]};
  asm volatile("global_store_dwordx2 %0, %1, off sc1\n\ts_nop 1" :: "v"(p), "v"(r) : "memory");
}

#define DMA4(base) \
      "global_load_lds_dwordx4 %[v0], " base " nt\n\t" \
      "global_load_lds_dwordx4 %[v1], " base " offset:1024 nt\n\t" \
      "global_load_lds_dwordx4 %[v2], " base " offset:2048 nt\n\t" \
      "global_load_lds_dwordx4 %[v3], " base " offset:3072 nt\n\t"

__device__ __forceinline__ void dma_tile(const char* pa, const char* pb, unsigned lds_dst, unsigned v0, unsigned v1, unsigned v2, unsigned v3) {
  unsigned keep;
  asm volatile(
      "s_mov_b32 %[keep], m0\n\t"
      "s_mov_b32 m0, %[dst]\n\t"
      "s_nop 0\n\t"
      DMA4("%[pa]")
      "s_add_u32 m0, m0, 0x1000\n\t"
      "s_nop 0\n\t"
      DMA4("%[pb]")
      "s_mov_b32 m0, %[keep]"
      : [keep] "=&s"(keep)
      : [v0] "v"(v0), [v1] "v"(v1), [v2] "v"(v2), [v3] "v"(v3), [pa] "s"(pa), [pb] "s"(pb), [dst] "s"(lds_dst)
      : "memory", "scc");
}
#define WAIT_VM(n) asm volatile("s_waitcnt vmcnt(" #n ")" ::: "memory")
#define BARRIER() asm volatile("s_barrier" ::: "memory")
#define WAIT_LGKM0() asm volatile("s_waitcnt lgkmcnt(0)" ::: "memory")

template <int G> __device__ __forceinline__ void wait_groups_const() {
  static_assert(G >= 0 && G <= 7, "vmcnt range");
  asm volatile("s_waitcnt vmcnt(%0)" :: "n"(8 * G) : "memory");
}

__global__ void __launch_bounds__(NT, 2)
attn_split(const __hip_bfloat16* __restrict__ q, const __hip_bfloat16* __restrict__ kc, const __hip_bfloat16* __restrict__ vc,
           const int* __restrict__ bt, const int* __restrict__ lens, unsigned char* __restrict__ ws,
           __hip_bfloat16* __restrict__ out) {
  __shared__ __attribute__((aligned(1024))) char lds[LDS_BYTES];
  const int tid = threadIdx.x, lane = tid & 63, wave = rfl(tid >> 6);
  const int hp = blockIdx.x & 3, h = hp * 2 + (wave & 1), j = blockIdx.x >> 2;
  const int u = j * 2 + (wave >> 1);
  if (wave == WAVES) {
    {
      constexpr int LN = 4;
      static_assert((size_t)16 * LN * ((8u << 20) / LN) <= (size_t)NPAGES * PAGE_BYTES, "touch range");
      const int r = (blockIdx.x >> 3) * LN + (lane & (LN - 1));
      const char* a = (const char*)(r < 16 * LN ? kc : vc) + (size_t)(r & (16 * LN - 1)) * ((8u << 20) / LN);
      const char* c = (const char*)ws + (size_t)(lane & 7) * HEAD_STRIDE + SLOTS_PER_HEAD * SLOT_BYTES;
      if (lane == 8) c = (const char*)bt;
      if (lane == 9) c = (const char*)q;
      if (lane == 10) c = (const char*)out;
      unsigned d0, d1, d2, d3;
      asm volatile("global_load_dword %2, %6, %7\n\tglobal_load_dword %3, %8, %7\n\t"
                   "global_load_dword %0, %4, off\n\tglobal_load_dword %1, %5, off\n\ts_waitcnt vmcnt(0)"
                   : "=&v"(d0), "=&v"(d1), "=&v"(d2), "=&v"(d3) : "v"(a), "v"(c), "v"(lane * 128), "s"(bt), "v"(lane * 128 + 8192) : "memory");
    }
    gu64* epoch = (gu64*)(ws + WS_EPOCH + hp * 256);
    const unsigned long long e0 = __hip_atomic_load(epoch, __ATOMIC_RELAXED, __HIP_MEMORY_SCOPE_AGENT);
    const Part ps = partition(lens, lane);
    const unsigned tag_lo = rfl((unsigned)e0) * 0x9E3779B1u + 0x7F4A7C15u;
    const unsigned tag_hi = (rfl((unsigned)(e0 >> 32)) ^ tag_lo) * 0x85EBCA6Bu + 0xC2B2AE35u;
    const unsigned long long tag = ((unsigned long long)tag_hi << 32) | tag_lo;
    const int rb = j >> 3;
    const int rj0 = __builtin_amdgcn_readlane(ps.start, rb) >> 1;
    const int rj1 = ((rb == B - 1 ? WPH : __builtin_amdgcn_readlane(ps.start, (rb + 1) & 7)) - 1) >> 1;
    char* flags = (char*)ws + WS_FLAG + hp * (WGPH * FLAG_STRIDE);
    BARRIER();
    BARRIER();
    if (lane == 0) __hip_atomic_store((gu64*)(flags + j * FLAG_STRIDE), tag, __ATOMIC_RELAXED, __HIP_MEMORY_SCOPE_AGENT);
    const int lo = j == 0 ? 0 : rj0, hi = j == 0 ? WGPH - 1 : rj1;
    {
      const char* f = flags + min(lo + lane, hi) * FLAG_STRIDE;
      const unsigned long long want = hi - lo == 63 ? ~0ull : (2ull << (hi - lo)) - 1;
      unsigned long long x;
      unsigned spins = SPIN_CAP;
      asm volatile(
          "s_mov_b64 exec, %[want]\n"
          ".Lpoll_loop%=:\n\t"
          "global_load_dwordx2 %[x], %[f], off sc1\n\t"
          "s_waitcnt vmcnt(0)\n\t"
          "v_cmp_ne_u64 vcc, %[tag], %[x]\n\t"
          "s_and_b64 exec, exec, vcc\n\t"
          "s_cbranch_execz .Lpoll_done%=\n\t"
          "s_sleep %[gap]\n\t"
          "s_sub_u32 %[spins], %[spins], 1\n\t"
          "s_cmp_lg_u32 %[spins], 0\n\t"
          "s_cbranch_scc1 .Lpoll_loop%=\n"
          ".Lpoll_done%=:\n\t"
          "s_mov_b64 exec, -1\n\t"
          "s_barrier"
          : [x] "=&v"(x), [spins] "+s"(spins)
          : [f] "v"(f), [tag] "s"(tag), [want] "s"(want), [gap] "n"(2)
          : "memory", "vcc", "scc");
    }
    if (j == 0 && lane == 0) __hip_atomic_store(epoch, tag, __ATOMIC_RELAXED, __HIP_MEMORY_SCOPE_AGENT);
    return;
  }

  const int h2 = lane >> 5, r32 = lane & 31;
  unsigned voff[4];
#pragma unroll
  for (int i = 0; i < 4; ++i) {
    const unsigned rr = lane >> 4;
    voff[i] = (4 * i + rr) * (HKV * D * 2) + 16 * ((lane & 15) ^ ((rr << 2) | i)) - 1024 * i;
  }
  unsigned qoff = (h * (HQ / HKV) * SQ + r32) * (D * 2) + h2 * 16;
  asm volatile("" : "+v"(voff[0]), "+v"(voff[1]), "+v"(voff[2]), "+v"(voff[3]), "+v"(qoff));

  Part p = partition(lens, lane);
  const int b = rfl(batch_of_wave(p, lane, u));
  const int s0 = __builtin_amdgcn_readlane(p.start, b);
  const int s1 = b == B - 1 ? WPH : __builtin_amdgcn_readlane(p.start, (b + 1) & 7);
  const int ntb = __builtin_amdgcn_readlane(p.nt, b);
  const int nw = s1 - s0, iw = u - s0;
  const float rn = (float)ntb * __builtin_amdgcn_rcpf((float)nw);
  const int t0 = rfl((int)((float)iw * rn));

  const unsigned lds0 = (unsigned)(size_t)(LDS_AS char*)lds + wave * WAVE_LDS;
  const char* kh = (const char*)kc + h * (D * 2);
  const char* vh = (const char*)vc + h * (D * 2);
  int btv;
  auto page = [&](const char* base, int logical) {
    return base + (size_t)__builtin_amdgcn_readlane(btv, logical) * PAGE_BYTES;
  };
#define ISSUE(base, tile, ldsoff) dma_tile(page(base, 2 * (tile)), page(base, 2 * (tile) + 1), lds0 + (ldsoff), voff[0], voff[1], voff[2], voff[3])

  u32x4 qf[8];
  {
    const char* qb = (const char*)q + b * (HQ * SQ * D * 2);
    const int* btb = bt + b * NBPS;
    const unsigned bo = min(2 * t0 + lane, NBPS - 1) * 4;
    const unsigned long long ku = (unsigned long long)kh, vu = (unsigned long long)vh;
    unsigned keep;
    asm volatile(
        "global_load_dword %[btv], %[bo], %[bt]\n\t"
        "global_load_dwordx4 %[q0], %[qo], %[qb]\n\t"
        "global_load_dwordx4 %[q1], %[qo], %[qb] offset:32\n\t"
        "global_load_dwordx4 %[q2], %[qo], %[qb] offset:64\n\t"
        "global_load_dwordx4 %[q3], %[qo], %[qb] offset:96\n\t"
        "global_load_dwordx4 %[q4], %[qo], %[qb] offset:128\n\t"
        "global_load_dwordx4 %[q5], %[qo], %[qb] offset:160\n\t"
        "global_load_dwordx4 %[q6], %[qo], %[qb] offset:192\n\t"
        "global_load_dwordx4 %[q7], %[qo], %[qb] offset:224\n\t"
        "s_mov_b32 %[keep], m0\n\t"
        "s_mov_b32 m0, %[dst]\n\t"
        "s_waitcnt vmcnt(8)\n\t"
        "v_readlane_b32 s64, %[btv], 0\n\t"
        "v_readlane_b32 s65, %[btv], 1\n\t"
        "s_lshr_b32 s61, s64, 17\n\t"
        "s_lshl_b32 s60, s64, 15\n\t"
        "s_lshr_b32 s63, s65, 17\n\t"
        "s_lshl_b32 s62, s65, 15\n\t"
        "s_add_u32 s64, s60, %[klo]\n\t"
        "s_addc_u32 s65, s61, %[khi]\n\t"
        "s_add_u32 s66, s62, %[klo]\n\t"
        "s_addc_u32 s67, s63, %[khi]\n\t"
        DMA4("s[64:65]")
        "s_add_u32 m0, m0, 0x1000\n\t"
        "s_nop 0\n\t"
        DMA4("s[66:67]")
        "s_add_u32 s60, s60, %[vlo]\n\t"
        "s_addc_u32 s61, s61, %[vhi]\n\t"
        "s_add_u32 s62, s62, %[vlo]\n\t"
        "s_addc_u32 s63, s63, %[vhi]\n\t"
        "s_add_u32 m0, m0, %[vdelta]\n\t"
        "s_nop 0\n\t"
        DMA4("s[60:61]")
        "s_add_u32 m0, m0, 0x1000\n\t"
        "s_nop 0\n\t"
        DMA4("s[62:63]")
        "s_mov_b32 m0, %[keep]\n\t"
        "s_waitcnt vmcnt(16)"
        : [btv] "=&v"(btv), [q0] "=&v"(qf[0]), [q1] "=&v"(qf[1]), [q2] "=&v"(qf[2]), [q3] "=&v"(qf[3]),
          [q4] "=&v"(qf[4]), [q5] "=&v"(qf[5]), [q6] "=&v"(qf[6]), [q7] "=&v"(qf[7]), [keep] "=&s"(keep)
        : [bo] "v"(bo), [bt] "s"(btb), [qo] "v"(qoff), [qb] "s"(qb), [v0] "v"(voff[0]), [v1] "v"(voff[1]), [v2] "v"(voff[2]), [v3] "v"(voff[3]),
          [klo] "s"((unsigned)ku), [khi] "s"((unsigned)(ku >> 32)), [vlo] "s"((unsigned)vu), [vhi] "s"((unsigned)(vu >> 32)),
          [dst] "s"(lds0), [vdelta] "n"(4096)
        : "memory", "scc", "s60", "s61", "s62", "s63", "s64", "s65", "s66", "s67");
  }
  asm volatile("" : "+v"(p.start), "+v"(p.len), "+v"(p.zeroq));
  const int t1 = rfl(iw + 1 == nw ? ntb : (int)((float)(iw + 1) * rn));
  const int n = t1 - t0;
  const int len = __builtin_amdgcn_readlane(p.len, b), zeroq = __builtin_amdgcn_readlane(p.zeroq, b);
  const bool same_batch = rfl(batch_of_wave(p, lane, u ^ 1)) == b;
  const int rb = j >> 3;
  const int rj0 = __builtin_amdgcn_readlane(p.start, rb) >> 1;
  const int rj1 = ((rb == B - 1 ? WPH : __builtin_amdgcn_readlane(p.start, (rb + 1) & 7)) - 1) >> 1;
  if (zeroq) {
#pragma unroll
    for (int s = 0; s < 8; ++s) qf[s] = u32x4{0u, 0u, 0u, 0u};
  }

  const int half = wave >> 1;
  const unsigned fr = ((r32 & 3) << 2) | ((r32 >> 2) & 3);
  const unsigned kbase = wave * WAVE_LDS + 256 * r32 + 16 * (h2 ^ fr);
  const unsigned qq = (lane & 15) >> 2, pp = lane & 3, blk = (lane >> 4) & 1;
  const unsigned vbase = wave * WAVE_LDS + 8192 + 256 * (4 * h2 + qq) + (16 * ((qq << 2) | ((2 * blk + (pp >> 1)) ^ h2)) ^ (128 * half)) + 8 * (pp & 1);

  f32x16 o[4];
#pragma unroll
  for (int c = 0; c < 4; ++c)
#pragma unroll
    for (int r = 0; r < 16; ++r) o[c][r] = 0.f;
  float m = -INFINITY, l = 0.f;
  const float span = fmaxf((float)p.total * (PACE_TILE * 16.f) - PACE_LEAD * 16.f, 0.f) + (float)(wave * 64 + j) * (PACE_SPREAD * 16.f / 256.f);
  const unsigned step = rfl((unsigned)(span * __builtin_amdgcn_rcpf((float)max(n - 2, 1))));
  unsigned due = PACE_FIRST * 16;

  for (int i = 0; i < n; ++i) {
    const int rem = n - 1 - i;
    wait_groups_const<1>();
    unsigned now = (unsigned)__builtin_amdgcn_s_memrealtime();
    bf16x8 kf[8];
#pragma unroll
    for (int s = 0; s < 8; ++s) kf[s] = *(const LDS_AS bf16x8*)((LDS_AS char*)lds + (kbase ^ (32 * s)));
    WAIT_LGKM0();
    if (rem >= 1) {
      while (now - p.tstart < (due >> 4)) {
        __builtin_amdgcn_s_sleep(3);
        now = (unsigned)__builtin_amdgcn_s_memrealtime();
      }
      due += step;
      ISSUE(kh, i + 1, 0);
    }
    f32x16 s;
#pragma unroll
    for (int r = 0; r < 16; ++r) s[r] = 0.f;
#pragma unroll
    for (int k = 0; k < 8; ++k) s = __builtin_amdgcn_mfma_f32_32x32x16_bf16(kf[k], __builtin_bit_cast(bf16x8, qf[k]), s, 0, 0, 0);
    const int key0 = (t0 + i) * 32;
    if (key0 + 32 > len) {
      const int lim = len - key0 - 4 * h2;
#pragma unroll
      for (int r = 0; r < 16; ++r) s[r] = ((r & 3) + 8 * (r >> 2) < lim) ? s[r] : -INFINITY;
    }
    float tm = s[0];
#pragma unroll
    for (int r = 1; r < 16; ++r) tm = fmaxf(tm, s[r]);
    tm = swap32_max(tm);
    const float mn = fmaxf(m, tm);
    const float alpha = ex2((m - mn) * C2), mc = mn * C2;
    m = mn;
    float ps = 0.f;
#pragma unroll
    for (int r = 0; r < 16; ++r) { s[r] = ex2(fmaf(s[r], C2, -mc)); ps += s[r]; }
    l = fmaf(l, alpha, ps);
#pragma unroll
    for (int c = 0; c < 4; ++c)
#pragma unroll
      for (int r = 0; r < 16; ++r) o[c][r] *= alpha;
    bf16x8 pf[2];
#pragma unroll
    for (int s2 = 0; s2 < 2; ++s2)
#pragma unroll
      for (int e = 0; e < 8; ++e) pf[s2][e] = (__bf16)s[8 * s2 + e];

    if (rem >= 1) wait_groups_const<1>();
    else wait_groups_const<0>();
    bf16x4 vf[4][2][2];
#pragma unroll
    for (int c = 0; c < 4; ++c)
#pragma unroll
      for (int t = 0; t < 2; ++t)
#pragma unroll
        for (int s2 = 0; s2 < 2; ++s2)
          vf[c][s2][t] = __builtin_amdgcn_ds_read_tr16_b64_v4bf16(
              (LDS_AS bf16x4*)((LDS_AS char*)lds + ((vbase ^ (64 * c) ^ (32 * t)) + 2048 * t + 4096 * s2)));
    WAIT_LGKM0();
    if (rem >= 1) ISSUE(vh, i + 1, 8192);
#pragma unroll
    for (int c = 0; c < 4; ++c)
#pragma unroll
      for (int s2 = 0; s2 < 2; ++s2) {
        const bf16x8 a = __builtin_shufflevector(vf[c][s2][0], vf[c][s2][1], 0, 1, 2, 3, 4, 5, 6, 7);
        o[c] = __builtin_amdgcn_mfma_f32_32x32x16_bf16(a, pf[s2], o[c], 0, 0, 0);
      }
  }
  WAIT_VM(0);

  l = swap32_sum(l);
  const float mc = m * C2;
  {
    LDS_AS char* mine = (LDS_AS char*)lds + wave * WAVE_LDS + lane * 16;
#pragma unroll
    for (int c = 0; c < 2; ++c)
#pragma unroll
      for (int i = 0; i < 4; ++i) {
        const f32x16& src = o[2 + c];
        *(LDS_AS f32x4*)(mine + (c * 4 + i) * 1024) = f32x4{src[4 * i], src[4 * i + 1], src[4 * i + 2], src[4 * i + 3]};
      }
    if (h2 == 0) *(LDS_AS f32x2*)((LDS_AS char*)lds + LDS_ML + wave * 256 + r32 * 8) = f32x2{mc, l};
  }
  __syncthreads();
  const int slot = h * SLOTS_PER_HEAD + j + b;
  const __amdgpu_buffer_rsrc_t rs = __builtin_amdgcn_make_buffer_rsrc(ws, 0, (int)WS_BYTES, 0x00020000);
  char* wo = (char*)(ws + WS_O) + (size_t)h * HEAD_STRIDE + (size_t)(j + b) * SLOT_BYTES + lane * 8;
  const int so_ml = (int)WS_ML + slot * (M * 8);
  if (same_batch) {
    const f32x2 ml = *(const LDS_AS f32x2*)((LDS_AS char*)lds + LDS_ML + (wave ^ 2) * 256 + r32 * 8);
    const float Mx = fmaxf(mc, ml[0]);
    const float fa = l > 0.f ? ex2(mc - Mx) : 0.f, fb = ml[1] > 0.f ? ex2(ml[0] - Mx) : 0.f;
    const LDS_AS char* other = (const LDS_AS char*)lds + (wave ^ 2) * WAVE_LDS + lane * 16;
#pragma unroll
    for (int c = 0; c < 2; ++c)
#pragma unroll
      for (int i = 0; i < 4; ++i) {
        const f32x16& src = o[c];
        const f32x4 x = *(const LDS_AS f32x4*)(other + (c * 4 + i) * 1024);
        const f32x4 y = f32x4{src[4 * i], src[4 * i + 1], src[4 * i + 2], src[4 * i + 3]};
        store_partial(wo + (half * 8 + c * 4 + i) * UNIT_BYTES, fa * y + fb * x);
      }
    if (half == 0 && h2 == 0)
      __builtin_amdgcn_raw_buffer_store_b64(__builtin_bit_cast(u32x2, f32x2{Mx, fmaf(fa, l, fb * ml[1])}), rs, r32 * 8, so_ml, AUX_SC1);
  } else {
#pragma unroll
    for (int c = 0; c < 4; ++c)
#pragma unroll
      for (int i = 0; i < 4; ++i)
        store_partial(wo + ((half ^ (c >> 1)) * 8 + (c & 1) * 4 + i) * UNIT_BYTES, f32x4{o[c][4 * i], o[c][4 * i + 1], o[c][4 * i + 2], o[c][4 * i + 3]});
    if (h2 == 0) __builtin_amdgcn_raw_buffer_store_b64(__builtin_bit_cast(u32x2, f32x2{mc, l}), rs, r32 * 8, so_ml, AUX_SC1);
  }
  WAIT_VM(0);
  BARRIER();
  BARRIER();
  {
    const int unit = (j & 7) * 4 + wave, rh = hp * 2 + (unit >> 4), ci = unit & 15;
    const int first = rb + rj0, cnt = rj1 - rj0 + 1;
    const __amdgpu_buffer_rsrc_t ro = __builtin_amdgcn_make_buffer_rsrc(
        ws + (size_t)rh * HEAD_STRIDE + (size_t)first * SLOT_BYTES, 0, cnt * SLOT_BYTES, 0x00020000);
    const __amdgpu_buffer_rsrc_t rm = __builtin_amdgcn_make_buffer_rsrc(
        ws + WS_ML + (size_t)(rh * SLOTS_PER_HEAD + first) * (M * 8), 0, cnt * (M * 8), 0x00020000);
    int vo = lane * 8 + ci * UNIT_BYTES, vml = r32 * 8;
    float Mx = -INFINITY, L = 0.f;
    f32x4 acc = {0.f, 0.f, 0.f, 0.f};
    for (int done = 0; done < cnt; done += CMB, vo += CMB * SLOT_BYTES, vml += CMB * (M * 8)) {
      f32x2 ml[CMB]; u32x2 w[CMB];
#pragma unroll
      for (int e = 0; e < CMB; ++e) {
        ml[e] = __builtin_bit_cast(f32x2, __builtin_amdgcn_raw_buffer_load_b64(rm, vml + e * (M * 8), 0, AUX_SC1));
        w[e] = __builtin_bit_cast(u32x2, __builtin_amdgcn_raw_buffer_load_b64(ro, vo + e * SLOT_BYTES, 0, AUX_SC1));
      }
      float mn = Mx;
#pragma unroll
      for (int e = 0; e < CMB; ++e) if (ml[e][1] > 0.f) mn = fmaxf(mn, ml[e][0]);
      const float a = L > 0.f ? ex2(Mx - mn) : 0.f;
      L *= a; acc *= a; Mx = mn;
#pragma unroll
      for (int e = 0; e < CMB; ++e) {
        const float f = ml[e][1] > 0.f ? ex2(ml[e][0] - mn) : 0.f;
        L = fmaf(f, ml[e][1], L);
        acc += f * f32x4{__uint_as_float(w[e][0] << 16), __uint_as_float(w[e][0] & 0xffff0000u),
                         __uint_as_float(w[e][1] << 16), __uint_as_float(w[e][1] & 0xffff0000u)};
      }
    }
    const float inv = 1.f / L;
    bf16x4 r;
#pragma unroll
    for (int e = 0; e < 4; ++e) r[e] = (__bf16)(acc[e] * inv);
    *(bf16x4*)((char*)out + (((size_t)(rb * HQ + rh * (HQ / HKV)) * SQ + r32) * D + 32 * (ci >> 2) + 8 * (ci & 3) + 4 * h2) * 2) = r;
  }
}

}

extern "C" void kernel_launch(void* const* d_in, const int* in_sizes, int n_in, void* d_out, int out_size, void* d_ws,
                              size_t ws_size, hipStream_t stream) {
  const __hip_bfloat16* q = (const __hip_bfloat16*)d_in[0];
  const __hip_bfloat16* k = (const __hip_bfloat16*)d_in[1];
  const __hip_bfloat16* v = (const __hip_bfloat16*)d_in[2];
  const int* bt = (const int*)d_in[3];
  const int* lens = (const int*)d_in[4];
  attn_split<<<NWG, NT, 0, stream>>>(q, k, v, bt, lens, (unsigned char*)d_ws, (__hip_bfloat16*)d_out);
}
```
